```python
import torch

def _softmax(x, axis=-1):
    return torch.softmax(x, dim=axis)
def _gelu(x):
    return torch.nn.functional.gelu(x, approximate="tanh")
def _silu(x):
    return torch.nn.functional.silu(x)
def _sigmoid(x):
    return torch.sigmoid(x)
def _layernorm(x, axis=-1, eps=1e-5):
    m = x.mean(dim=axis, keepdim=True)
    v = x.var(dim=axis, keepdim=True, unbiased=False)
    return (x - m) / torch.sqrt(v + eps)
def _scatter_add(tgt, idx, src, axis=0):
    return tgt.clone().index_add_(axis, idx, src)
def _segment_sum(src, seg, n):
    out = torch.zeros((n,) + src.shape[1:], dtype=src.dtype)
    return out.index_add_(0, seg, src)
def _pad(x, pw):
    flat = tuple(v for pair in reversed(pw) for v in pair)
    return torch.nn.functional.pad(x, flat)

kernel_name = 'parallel_sum__sliding_block_cached__dense_activation_relu__decode'

def setup_inputs():

    _w = (lambda a: (lambda t: t.to(torch.bfloat16) if t.is_floating_point() else t)(torch.as_tensor(a)))
    def _A_setup():
        B, Sq, D = 8, 1, 512
        Hq, Hkv, Sk, Dh = 32, 8, 8192, 128
        assert Hq % Hkv == 0, (Hq, Hkv)
        return {
            "x": _w((lambda *s: torch.randn(*s, dtype=torch.bfloat16) * 0.05)(B, Sq, D)),
            "k_cache": _w((lambda *s: torch.randn(*s, dtype=torch.bfloat16) * 3.2)(B, Hkv, Sk, Dh)),
            "v_cache": _w((lambda *s: torch.randn(*s, dtype=torch.bfloat16) * 1.6)(B, Hkv, Sk, Dh)),
            "wq": _w((lambda *s: torch.randn(*s, dtype=torch.bfloat16) * 3.2)(D, Hq * Dh) * (D ** -0.5)),
            "wo": _w((lambda *s: torch.randn(*s, dtype=torch.bfloat16) * 1.6)(Hq * Dh, D) * ((Hq * Dh) ** -0.5)),
        }
    _A_in = _A_setup()

    def _B_setup():
        B, S, D, Dff = 8, 1, 512, 1792
        return {
            "x": _w((lambda *s: torch.randn(*s, dtype=torch.bfloat16) * 0.05)(B, S, D)),
            "w1": _w((lambda *s: torch.randn(*s, dtype=torch.bfloat16) * 0.05)(D, Dff)),
            "w2": _w((lambda *s: torch.randn(*s, dtype=torch.bfloat16) * 0.05)(Dff, D)),
        }
    _B_in = _B_setup()

    return {
        "A_x": _A_in["x"],
        "A_k_cache": _A_in["k_cache"],
        "A_v_cache": _A_in["v_cache"],
        "A_wq": _A_in["wq"],
        "A_wo": _A_in["wo"],
        "B_x": _B_in["x"],
        "B_w1": _B_in["w1"],
        "B_w2": _B_in["w2"],
    }

def reference(A_x, A_k_cache, A_v_cache, A_wq, A_wo, B_x, B_w1, B_w2):
    def _A_ref(x, k_cache, v_cache, wq, wo):
        B, Sq, D = x.shape
        Hq, Dh = 32, 128
        Hkv, Sk = k_cache.shape[1], k_cache.shape[2]
        W = min(4096, max(256, Sk // 4))
        rep = Hq // Hkv
        q = (lambda x, *d: x.permute(*d))((x @ wq).reshape(B, Sq, Hq, Dh), 0, 2, 1, 3)
        k = (lambda x, n, axis: torch.repeat_interleave(x, n, dim=axis))(k_cache[:, :, Sk - W:, :], rep, axis=1)
        v = (lambda x, n, axis: torch.repeat_interleave(x, n, dim=axis))(v_cache[:, :, Sk - W:, :], rep, axis=1)
        scores = torch.einsum("bhqd,bhkd->bhqk", q, k) * (Dh ** -0.5)
        p = _softmax(scores, axis=-1)
        o = torch.einsum("bhqk,bhkd->bhqd", p, v)
        return (lambda x, *d: x.permute(*d))(o, 0, 2, 1, 3).reshape(B, Sq, Hq * Dh) @ wo

    def _B_ref(x, w1, w2):
        h = torch.einsum("bsd,df->bsf", x, w1)
        zero = torch.zeros_like(h)
        h = torch.maximum(h, zero)
        return torch.einsum("bsf,fd->bsd", h, w2)

    x = A_x
    xn = _layernorm(x, axis=-1)
    return x + _A_ref(xn, A_k_cache, A_v_cache, A_wq, A_wo) + _B_ref(xn, B_w1, B_w2)
```

```cpp
#include <hip/hip_runtime.h>
#include <hip/hip_bf16.h>
#include <stdint.h>

#define MIMIC 0

typedef unsigned short u16;
__device__ __forceinline__ float bf2f(u16 b) { return __uint_as_float(((unsigned)b) << 16); }
__device__ __forceinline__ u16 f2bf(float f) { __hip_bfloat16 h = __float2bfloat16(f); return __builtin_bit_cast(u16, h); }
__device__ __forceinline__ float rbf(float f) { return bf2f(f2bf(f)); }

constexpr int B = 8, D = 512, HQ = 32, HKV = 8, SK = 8192, DH = 128, W = 2048, DFF = 1792;

__device__ void layernorm_to_lds(const u16* x, float* xn  ) {
  int wave = threadIdx.x >> 6, lane = threadIdx.x & 63;
  for (int r = wave; r < 8; r += blockDim.x / 64) {
    float v[8]; float s = 0.f;
    for (int i = 0; i < 8; ++i) { v[i] = bf2f(x[r * D + lane * 8 + i]); s += v[i]; }
    for (int o = 32; o; o >>= 1) s += __shfl_xor(s, o);
    float mean = s * (1.f / D);
    float q = 0.f;
    for (int i = 0; i < 8; ++i) { float d = v[i] - mean; q += d * d; }
    for (int o = 32; o; o >>= 1) q += __shfl_xor(q, o);
    float var = q * (1.f / D);
    float rs = 1.f / sqrtf(var + 1e-5f);
    for (int i = 0; i < 8; ++i) xn[r * D + lane * 8 + i] = rbf((v[i] - mean) * rs);
  }
}

__global__ void __launch_bounds__(256) k1(const u16* x, const u16* wq, const u16* w1, float* q, float* h) {
  __shared__ float xn[8 * D];
  layernorm_to_lds(x, xn);
  __syncthreads();
  int col = blockIdx.x * 256 + threadIdx.x;
  const u16* w; int ld; int c; float* o; int n;
  if (col < 4096) { w = wq; ld = 4096; c = col; o = q; } else { w = w1; ld = DFF; c = col - 4096; o = h; }
  float acc[8] = {0};
  for (int k = 0; k < D; ++k) {
    float wv = bf2f(w[(size_t)k * ld + c]);
    for (int b = 0; b < 8; ++b) acc[b] += xn[b * D + k] * wv;
  }
  for (int b = 0; b < 8; ++b) {
    float v = rbf(acc[b]);
    if (col >= 4096) v = fmaxf(v, 0.f);
    o[b * ld + c] = v;
  }
}

__global__ void __launch_bounds__(256) k2(const u16* kc, const u16* vc, const float* q, float* o) {
  __shared__ float s[W];
  __shared__ float qs[DH];
  __shared__ float red[8];
  __shared__ float part[2][DH];
  int b = blockIdx.x / HQ, h = blockIdx.x % HQ, kvh = h / 4;
  int tid = threadIdx.x;
  if (tid < DH) qs[tid] = q[b * 4096 + h * DH + tid];
  __syncthreads();
  const u16* kb = kc + ((size_t)(b * HKV + kvh) * SK + (SK - W)) * DH;
  const u16* vb = vc + ((size_t)(b * HKV + kvh) * SK + (SK - W)) * DH;
  float mx = -INFINITY;
  for (int k = tid; k < W; k += 256) {
    float a = 0.f;
    for (int d = 0; d < DH; ++d) a += qs[d] * bf2f(kb[(size_t)k * DH + d]);
    a = a * 0.08838834764831845f;
    s[k] = a; mx = fmaxf(mx, a);
  }
  for (int o2 = 32; o2; o2 >>= 1) mx = fmaxf(mx, __shfl_xor(mx, o2));
  if ((tid & 63) == 0) red[tid >> 6] = mx;
  __syncthreads();
  mx = fmaxf(fmaxf(red[0], red[1]), fmaxf(red[2], red[3]));
  float sum = 0.f;
  for (int k = tid; k < W; k += 256) { float e = __expf(s[k] - mx); s[k] = e; sum += e; }
  for (int o2 = 32; o2; o2 >>= 1) sum += __shfl_xor(sum, o2);
  if ((tid & 63) == 0) red[4 + (tid >> 6)] = sum;
  __syncthreads();
  sum = red[4] + red[5] + red[6] + red[7];
  float inv = 1.f / sum;
  int d = tid & 127, half = tid >> 7;
  float a = 0.f;
  for (int k = half * 1024; k < half * 1024 + 1024; ++k) {
    float p = s[k] * inv;
    a += p * bf2f(vb[(size_t)k * DH + d]);
  }
  part[half][d] = a;
  __syncthreads();
  if (tid < DH) o[b * 4096 + h * DH + tid] = rbf(part[0][tid] + part[1][tid]);
}

__global__ void __launch_bounds__(256) k3(const u16* x, const u16* wo, const u16* w2, const float* o, const float* h, u16* out) {
  __shared__ float pa[4][64], pb[4][64];
  int b = blockIdx.x / 8, cb = blockIdx.x % 8;
  int c = cb * 64 + (threadIdx.x & 63), g = threadIdx.x >> 6;
  float a = 0.f, f = 0.f;
  for (int k = g; k < 4096; k += 4) a += o[b * 4096 + k] * bf2f(wo[(size_t)k * D + c]);
  for (int k = g; k < DFF; k += 4) f += h[b * DFF + k] * bf2f(w2[(size_t)k * D + c]);
  pa[g][threadIdx.x & 63] = a; pb[g][threadIdx.x & 63] = f;
  __syncthreads();
  if (threadIdx.x < 64) {
    int t = threadIdx.x;
    float A = pa[0][t] + pa[1][t] + pa[2][t] + pa[3][t];
    float F = pb[0][t] + pb[1][t] + pb[2][t] + pb[3][t];
    float xv = bf2f(x[b * D + c]);
    out[b * D + c] = f2bf(xv + A + F);
  }
}

extern "C" void kernel_launch(void* const* d_in, const int* in_sizes, int n_in,
                              void* d_out, int out_size, void* d_ws, size_t ws_size,
                              hipStream_t stream) {
  const u16* x = (const u16*)d_in[0];
  const u16* kc = (const u16*)d_in[1];
  const u16* vc = (const u16*)d_in[2];
  const u16* wq = (const u16*)d_in[3];
  const u16* wo = (const u16*)d_in[4];
  const u16* w1 = (const u16*)d_in[6];
  const u16* w2 = (const u16*)d_in[7];
  float* q = (float*)d_ws;
  float* h = q + 8 * 4096;
  float* o = h + 8 * DFF;
  k1<<<(4096 + DFF) / 256, 256, 0, stream>>>(x, wq, w1, q, h);
  k2<<<B * HQ, 256, 0, stream>>>(kc, vc, q, o);
  k3<<<64, 256, 0, stream>>>(x, wo, w2, o, h, (u16*)d_out);
}
```

```cpp
#include <hip/hip_runtime.h>
#include <stdint.h>

typedef unsigned short u16;
typedef unsigned v4u __attribute__((ext_vector_type(4)));
typedef unsigned v2u __attribute__((ext_vector_type(2)));
typedef float f32x4 __attribute__((ext_vector_type(4)));
typedef float f32x2 __attribute__((ext_vector_type(2)));
typedef __bf16 bf16x8 __attribute__((ext_vector_type(8)));
typedef __bf16 bf16x2 __attribute__((ext_vector_type(2)));
typedef short v4i16 __attribute__((ext_vector_type(4)));
typedef int v4i __attribute__((ext_vector_type(4)));
#define LAS __attribute__((address_space(3)))
#define GAS __attribute__((address_space(1)))

#define STAMPS 0
#define W_TRIG 1
#define NO_PRO 0
#define NO_TAIL 0
#define THR 8
#define GATE 1
#define W_AUX 2
#define EARLY_BAR 0
#define HEADS_UP_VM 2

constexpr int NWAVE = 9;
constexpr int AUX_NT = 2, AUX_SC1 = 16;
constexpr int RSRC_W3 = 0x00020000;
#define EPOCH_OFF 0
constexpr int WS_EPOCH = EPOCH_OFF;
constexpr int WS_QG = 4096;
constexpr int WS_HG = WS_QG + 8 * 8 * 128 * 16;
constexpr int WS_LSE = WS_HG + 8 * 448 * 16;
constexpr int WS_H1 = WS_LSE + 256 * 128;
constexpr int WS_H2 = WS_H1 + 32 * 8 * 4 * 512;
constexpr int WS_END = WS_H2 + 128 * 32 * 128;
constexpr int L_K = 0;
constexpr int LK_ACC = 0, LK_P = 2048;
constexpr int L_W = 131072;
constexpr int L_Q = L_W + 24576;
constexpr int L_O = L_Q + 1024;
constexpr int L_H = L_O + 8 * 272;
constexpr int L_X = L_H + 8 * 144;
constexpr int L_ML = L_X + 1024;
constexpr int L_WT = L_ML + 256;
constexpr int L_FLAG = L_WT + 128;
constexpr int L_END = L_FLAG + 64;
enum { F_Q = 0, F_ISSUE = 1, F_SOFT = 2, F_NEAR = 3, F_W = 4, F_O = 5, F_X = 6, F_G = 8, F_N = 12 };

constexpr unsigned SPIN_G = 1u << 16;
constexpr unsigned SPIN_L = 1u << 20;

__device__ __forceinline__ float bflo(unsigned v) { return __uint_as_float(v << 16); }
__device__ __forceinline__ float bfhi(unsigned v) { return __uint_as_float(v & 0xffff0000u); }
__device__ __forceinline__ unsigned pk_bf16(float lo, float hi) {
  f32x2 f = {lo, hi};
  return __builtin_bit_cast(unsigned, __builtin_convertvector(f, bf16x2));
}
__device__ __forceinline__ float rbf(float x) { return bflo(pk_bf16(x, x)); }
__device__ __forceinline__ f32x4 mfma(v4u a, v4u b, f32x4 c) {
  return __builtin_amdgcn_mfma_f32_16x16x32_bf16(__builtin_bit_cast(bf16x8, a), __builtin_bit_cast(bf16x8, b), c, 0, 0, 0);
}
template <int CTRL> __device__ __forceinline__ float dppf(float x) {
  return __builtin_bit_cast(float, __builtin_amdgcn_mov_dpp(__builtin_bit_cast(int, x), CTRL, 0xf, 0xf, true));
}
template <int CTRL> __device__ __forceinline__ unsigned dppu(unsigned x) {
  return (unsigned)__builtin_amdgcn_mov_dpp((int)x, CTRL, 0xf, 0xf, true);
}
constexpr int XOR1 = 0xB1, XOR2 = 0x4E, HMIR = 0x141, ROR8 = 0x128;
__device__ __forceinline__ float fmax_raw(float a, float b) { return __builtin_amdgcn_fmed3f(a, b, __builtin_inff()); }
__device__ __forceinline__ float x16_32_sum(float x) {
  auto s = __builtin_amdgcn_permlane16_swap(__float_as_uint(x), __float_as_uint(x), false, false);
  x = __uint_as_float(s[0]) + __uint_as_float(s[1]);
  auto t = __builtin_amdgcn_permlane32_swap(__float_as_uint(x), __float_as_uint(x), false, false);
  return __uint_as_float(t[0]) + __uint_as_float(t[1]);
}
__device__ __forceinline__ float x32_sum(float x) {
  auto t = __builtin_amdgcn_permlane32_swap(__float_as_uint(x), __float_as_uint(x), false, false);
  return __uint_as_float(t[0]) + __uint_as_float(t[1]);
}
template <int AUX> __device__ __forceinline__ v4u bld(__amdgpu_buffer_rsrc_t r, int voff, int soff) {
  return __builtin_amdgcn_raw_buffer_load_b128(r, voff, soff, AUX);
}
__device__ __forceinline__ void bst_sc1(v4u v, __amdgpu_buffer_rsrc_t r, int voff) {
  __builtin_amdgcn_raw_buffer_store_b128(v, r, voff, 0, AUX_SC1);
}
__device__ __forceinline__ bool tag_ok(v4u v, unsigned tag) { return (v.y == tag) & (v.w == tag); }
#define CBAR() asm volatile("" ::: "memory")
#define LGKM0() asm volatile("s_waitcnt lgkmcnt(0)" ::: "memory")
#define VMCNT(n) asm volatile("s_waitcnt vmcnt(%0)" ::"i"(n) : "memory")
#define SCHED() __builtin_amdgcn_sched_barrier(0)
#define PIN(x) asm volatile("" : "+v"(x))
__device__ __forceinline__ void wg_barrier() { SCHED(); LGKM0(); __builtin_amdgcn_s_barrier(); CBAR(); SCHED(); }
template <int SLEEP> __device__ __forceinline__ void lds_wait_ge(LAS unsigned* p, unsigned want) {
#pragma unroll 1
  for (unsigned i = 0; i < SPIN_L; ++i) {
    unsigned v = __builtin_amdgcn_readfirstlane(*(volatile LAS unsigned*)p);
    if (v >= want) break;
    if (SLEEP) __builtin_amdgcn_s_sleep(SLEEP);
  }
  CBAR();
}
__device__ __forceinline__ void lds_signal(LAS unsigned* p, int lane) {
  CBAR();
  if (lane == 0) asm volatile("ds_add_u32 %0, %1" ::"v"((unsigned)(size_t)p), "v"(1u) : "memory");
  CBAR();
}
__device__ __forceinline__ v4i make_srd(const void* p, int bytes) {
  const unsigned long long a = (unsigned long long)p;
  return v4i{(int)(unsigned)a, (int)(unsigned)(a >> 32), bytes, RSRC_W3};
}
__device__ __forceinline__ void dma4(v4i srd, int v0, int v1, int v2, int v3, int soff, unsigned lds) {
  unsigned keep;
  asm volatile(
      "s_mov_b32 %0, m0\n\ts_mov_b32 m0, %7\n\ts_nop 0\n\t"
      "buffer_load_dwordx4 %1, %5, %6 offen nt lds\n\t"
      "buffer_load_dwordx4 %2, %5, %6 offen offset:1024 nt lds\n\t"
      "buffer_load_dwordx4 %3, %5, %6 offen offset:2048 nt lds\n\t"
      "buffer_load_dwordx4 %4, %5, %6 offen offset:3072 nt lds\n\t"
      "s_mov_b32 m0, %0"
      : "=&s"(keep) : "v"(v0), "v"(v1), "v"(v2), "v"(v3), "s"(srd), "s"(soff), "s"(lds) : "memory");
}
__device__ __forceinline__ void dma1(v4i srd, int v0, unsigned lds) {
  unsigned keep;
  asm volatile("s_mov_b32 %0, m0\n\ts_mov_b32 m0, %3\n\ts_nop 0\n\tbuffer_load_dwordx4 %1, %2, 0 offen nt lds\n\ts_mov_b32 m0, %0"
               : "=&s"(keep) : "v"(v0), "s"(srd), "s"(lds) : "memory");
}
__device__ __forceinline__ v4u tr_frag(LAS unsigned char* p, int rowstep4) {
  v2u a = __builtin_bit_cast(v2u, __builtin_amdgcn_ds_read_tr16_b64_v4i16((LAS v4i16*)p));
  v2u b = __builtin_bit_cast(v2u, __builtin_amdgcn_ds_read_tr16_b64_v4i16((LAS v4i16*)(p + rowstep4)));
  return v4u{a.x, a.y, b.x, b.y};
}

#define STAMP(i)

__global__ void __launch_bounds__(NWAVE * 64, 3)
fused_kernel(const u16* __restrict__ x, const u16* __restrict__ kc, const u16* __restrict__ vc, const u16* __restrict__ wq,
             const u16* __restrict__ wo, const u16* __restrict__ w1, const u16* __restrict__ w2, u16* __restrict__ out,
             unsigned char* __restrict__ ws) {
  __shared__ __attribute__((aligned(16))) unsigned char smem[L_END];
  LAS unsigned char* L = (LAS unsigned char*)smem;
  STAMP(0);
  asm volatile("" ::"s"(x), "s"(kc), "s"(vc), "s"(wq), "s"(wo), "s"(w1), "s"(w2), "s"(out), "s"(ws));
  const int wg = blockIdx.x, kvh = wg & 7, idx = wg >> 3, b = idx & 7, s = idx >> 3;
  const int lane = threadIdx.x & 63, wave = __builtin_amdgcn_readfirstlane(threadIdx.x >> 6);
  const int c = lane & 15, g = lane >> 4;
  const __amdgpu_buffer_rsrc_t rws = __builtin_amdgcn_make_buffer_rsrc((void*)ws, 0, WS_END, RSRC_W3);
  LAS unsigned* flags = (LAS unsigned*)(L + L_FLAG);
  const int h2 = kvh * 4 + (idx & 3), cb8 = idx >> 2;
  const unsigned epoch_v = __builtin_amdgcn_raw_buffer_load_b32(rws, WS_EPOCH, 0, AUX_SC1);

  if (wave == 8) {
    if (lane < F_N) flags[lane] = 0u;
    v4u warm = bld<AUX_SC1>(rws, WS_LSE + kvh * 4096 + (lane & 31) * 128, 0);
    const int set = wg >> 1, ocol = 64 * (set >> 4) + 4 * (set & 15);
    const v2u xres = *(const GAS v2u*)(x + (lane & 7) * 512 + ocol);
    wg_barrier();
    const unsigned tag = __builtin_amdgcn_readfirstlane(epoch_v) + 1u;
    PIN(warm);
    lds_wait_ge<8>(&flags[F_Q], 2u);
    __builtin_amdgcn_s_setprio(3);
    if (!NO_PRO && !NO_TAIL) {
      const int u = lane & 7, hb = lane >> 3;
      const int src = WS_HG + (hb * 448 + 14 * h2 + u) * 16;
      v4u h0, h1;
#pragma unroll 1
      for (unsigned it = 0; it < SPIN_G; ++it) {
        CBAR();
        h0 = bld<AUX_SC1>(rws, src, 0);
        h1 = bld<AUX_SC1>(rws, u < 6 ? src + 128 : src, 0);
        if (__all(tag_ok(h0, tag) & tag_ok(h1, tag))) break;
        __builtin_amdgcn_s_sleep(8);
      }
      __builtin_amdgcn_s_setprio(0);
      *(LAS v2u*)(L + L_H + hb * 144 + u * 8) = v2u{h0.x, h0.z};
      *(LAS v2u*)(L + L_H + hb * 144 + 64 + u * 8) = u < 6 ? v2u{h1.x, h1.z} : v2u{0u, 0u};
    }
    STAMP(1);
    lds_wait_ge<2>(&flags[F_SOFT], 8u);
    unsigned lse0, lse1;
    {
      const float m = *(LAS float*)(L + L_ML + (lane & 31) * 4), l = *(LAS float*)(L + L_ML + 128 + (lane & 31) * 4);
      float M = fmax_raw(m, dppf<XOR1>(m)); M = fmax_raw(M, dppf<XOR2>(M)); M = fmax_raw(M, dppf<HMIR>(M));
      const float e = __builtin_amdgcn_exp2f(m - M);
      float Ls = e * l; Ls += dppf<XOR1>(Ls); Ls += dppf<XOR2>(Ls); Ls += dppf<HMIR>(Ls);
      *(LAS float*)(L + L_WT + (lane & 31) * 4) = e * __builtin_amdgcn_rcpf(Ls);
      lse0 = __float_as_uint(M + __builtin_amdgcn_logf(Ls));
      lse1 = dppu<ROR8>(lse0);
    }
    STAMP(2);
    lds_wait_ge<0>(&flags[F_NEAR], 8u);
    __builtin_amdgcn_s_setprio(3);
    if (!NO_TAIL && (lane == 0 || lane == 16)) bst_sc1(v4u{lse0, tag, lse1, tag}, rws, WS_LSE + ((kvh * 8 + b) * 4 + s) * 128 + lane);
    STAMP(3);
    wg_barrier();
    STAMP(7);
    if (NO_TAIL) { if (wg == 0 && lane == 0) { __builtin_amdgcn_s_sleep(127); __builtin_amdgcn_s_sleep(127); __builtin_amdgcn_raw_buffer_store_b32(tag, rws, WS_EPOCH, 0, AUX_SC1); } }
    else if (!(wg & 1)) {
      const int src = WS_H2 + set * 4096 + lane * 16;
      v4u u[4];
      bool ok = false;
#pragma unroll 1
      for (unsigned it = 0; it < SPIN_G; ++it) {
        CBAR();
        if (!ok) {
#pragma unroll
          for (int n = 0; n < 4; ++n) u[n] = bld<AUX_SC1>(rws, src + n * 1024, 0);
          ok = tag_ok(u[0], tag) & tag_ok(u[1], tag) & tag_ok(u[2], tag) & tag_ok(u[3], tag);
        }
        if (__all(ok)) break;
      }
      STAMP(9);
      float a[4];
      a[0] = (bflo(u[0].x) + bflo(u[1].x)) + (bflo(u[2].x) + bflo(u[3].x));
      a[1] = (bfhi(u[0].x) + bfhi(u[1].x)) + (bfhi(u[2].x) + bfhi(u[3].x));
      a[2] = (bflo(u[0].z) + bflo(u[1].z)) + (bflo(u[2].z) + bflo(u[3].z));
      a[3] = (bfhi(u[0].z) + bfhi(u[1].z)) + (bfhi(u[2].z) + bfhi(u[3].z));
#pragma unroll
      for (int k = 0; k < 4; ++k) { a[k] += dppf<ROR8>(a[k]); a[k] = x16_32_sum(a[k]); }
      if (lane < 8)
        *(GAS v2u*)(out + lane * 512 + ocol) = v2u{pk_bf16(bflo(xres.x) + a[0], bfhi(xres.x) + a[1]), pk_bf16(bflo(xres.y) + a[2], bfhi(xres.y) + a[3])};
      if (wg == 0 && lane == 0) __builtin_amdgcn_raw_buffer_store_b32(tag, rws, WS_EPOCH, 0, AUX_SC1);
      STAMP(10);
    }
  } else {
    LAS unsigned char* LK = L + L_K + wave * 16384;
    const bool isq = !(idx & 1);
    const bool do_pro = !NO_PRO && (wave == 4 || wave == 5) && (isq || idx < 28);
    if (do_pro) {
      __builtin_amdgcn_s_setprio(3);
      const int rh = wave - 4;
      const int ldb = isq ? 8192 : 3584;
      const int colb = isq ? kvh * 1024 + (idx >> 1) * 64 : (kvh * 14 + (idx >> 1)) * 32;
      const __amdgpu_buffer_rsrc_t rx = __builtin_amdgcn_make_buffer_rsrc((void*)x, 0, 8192, RSRC_W3);
      const __amdgpu_buffer_rsrc_t rw = __builtin_amdgcn_make_buffer_rsrc((void*)(isq ? wq : w1), 0, 512 * ldb, RSRC_W3);
      v4u xr[8], wr[16];
      const int xoff = (c & 7) * 1024 + (c >> 3) * 256 + g * 16;
#pragma unroll
      for (int t = 0; t < 8; ++t) xr[t] = bld<0>(rx, xoff + (t & 3) * 64, (rh ^ (t >> 2)) * 512);
      const int woff = (!isq && (lane & 2)) ? 0x40000000 : (rh * 256 + (lane >> 2)) * ldb + colb + (lane & 3) * 16;
#pragma unroll
      for (int i = 0; i < 16; ++i) wr[i] = bld<W_AUX>(rw, woff, i * 16 * ldb);
      wg_barrier();
      SCHED();
      __builtin_amdgcn_s_setprio(0);
      const unsigned tag = __builtin_amdgcn_readfirstlane(epoch_v) + 1u;
      float sum = 0.f;
#pragma unroll
      for (int t = 0; t < 8; ++t)
#pragma unroll
        for (int k = 0; k < 4; ++k) sum += bflo(xr[t][k]) + bfhi(xr[t][k]);
      sum += dppf<ROR8>(sum);
      sum = x16_32_sum(sum);
      const float mean = sum * (1.f / 512.f);
      float sq = 0.f;
#pragma unroll
      for (int t = 0; t < 8; ++t)
#pragma unroll
        for (int k = 0; k < 4; ++k) { float d0 = bflo(xr[t][k]) - mean, d1 = bfhi(xr[t][k]) - mean; sq = fmaf(d0, d0, sq); sq = fmaf(d1, d1, sq); }
      sq += dppf<ROR8>(sq);
      sq = x16_32_sum(sq);
      const float mb = rbf(mean), vb = rbf(sq * (1.f / 512.f));
      const float den = rbf(sqrtf(rbf(vb + 1e-5f)));
      const float rden = 1.0f / den;
      v4u xb[4];
#pragma unroll
      for (int t = 0; t < 4; ++t)
#pragma unroll
        for (int k = 0; k < 4; ++k) {
          unsigned d = pk_bf16(bflo(xr[t][k]) - mb, bfhi(xr[t][k]) - mb);
          xb[t][k] = pk_bf16(bflo(d) * rden, bfhi(d) * rden);
        }
      SCHED();
      STAMP(1);
#pragma unroll
      for (int i = 0; i < 16; ++i) *(LAS v4u*)(LK + i * 1024 + lane * 16) = wr[i];
      LGKM0();
      LAS unsigned char* tb = LK + (8 * g + (c >> 2)) * 64 + (c & 3) * 8;
      f32x4 z[2];
#pragma unroll
      for (int ct = 0; ct < 2; ++ct) {
        f32x4 a1 = {0.f, 0.f, 0.f, 0.f}, a2 = {0.f, 0.f, 0.f, 0.f};
#pragma unroll
        for (int t = 0; t < 4; ++t) {
          a1 = mfma(tr_frag(tb + ct * 32 + t * 2048, 256), xb[t], a1);
          a2 = mfma(tr_frag(tb + ct * 32 + (t + 4) * 2048, 256), xb[t], a2);
        }
#pragma unroll
        for (int r = 0; r < 4; ++r) { const float v = (c & 8) ? a2[r] : a1[r]; z[ct][r] = v + dppf<ROR8>(v); }
      }
      f32x4 zz;
#pragma unroll
      for (int r = 0; r < 4; ++r) zz[r] = (c & 8) ? z[1][r] : z[0][r];
      if (rh) {
        *(LAS f32x4*)(L + L_X + lane * 16) = zz;
        lds_signal(&flags[F_X], lane);
      } else {
        lds_wait_ge<1>(&flags[F_X], 1u);
        zz += *(LAS f32x4*)(L + L_X + lane * 16);
        if (!isq) {
#pragma unroll
          for (int r = 0; r < 4; ++r) zz[r] = fmaxf(zz[r], 0.f);
        }
        const int dst = (isq ? WS_QG + (kvh * 8 + (c & 7)) * 2048 + (idx >> 1) * 128 : WS_HG + (c & 7) * 7168 + colb * 2) + ((c >> 3) * 4 + g) * 16;
        v4u pv = v4u{pk_bf16(zz[0], zz[1]), tag, pk_bf16(zz[2], zz[3]), tag};
        PIN(pv);
        SCHED();
        __builtin_amdgcn_s_setprio(3);
        if (isq || c < 8) bst_sc1(pv, rws, dst);
        SCHED();
        __builtin_amdgcn_s_setprio(0);
      }
      LGKM0();
      STAMP(2);
    }

    const size_t kvbase = ((size_t)(b * 8 + kvh) * 8192 + 6144 + s * 512) * 128;
    const v4i rk = make_srd(kc + kvbase, 131072);
    const __amdgpu_buffer_rsrc_t rv = __builtin_amdgcn_make_buffer_rsrc((void*)(vc + kvbase), 0, 131072, RSRC_W3);
    const int rot = idx + wave;
    v4u Vf[2][8];
    int voff0 = ((8 * g + 16 * rot) & 63) * 256 + c * 16, voff1 = voff0 ^ 8192;
    {
      int k0 = g * 256 + ((c ^ g) << 4), k1 = k0 ^ 64, k2 = k0 ^ 128, k3 = k0 ^ 192;
      const unsigned lk = (unsigned)(size_t)LK;
      asm volatile("" : "+v"(k0), "+v"(k1), "+v"(k2), "+v"(k3), "+v"(voff0), "+v"(voff1) : "s"(rk), "s"(lk));
      if (wave >= 4) __builtin_amdgcn_s_setprio(1);
      if (!do_pro) wg_barrier();
#pragma unroll
      for (int tau = 0; tau < 4; ++tau) dma4(rk, k0, k1, k2, k3, wave * 16384 + ((tau + rot) & 3) * 4096, lk + tau * 4096);
      SCHED();
    }
#define V_LOADS() do { _Pragma("unroll") for (int i = 0; i < 16; ++i) Vf[i >> 3][i & 7] = bld<AUX_NT>(rv, ((i & 8) ? voff1 : voff0) + (i & 7) * 256, wave * 16384); \
      SCHED(); lds_signal(&flags[F_ISSUE], lane); STAMP(4); } while (0)
    unsigned tag;
    if (wave < 4) {
      V_LOADS();
      VMCNT(16);
      tag = __builtin_amdgcn_readfirstlane(epoch_v) + 1u;
    } else {
      VMCNT(0);
      tag = __builtin_amdgcn_readfirstlane(epoch_v) + 1u;
      if (NO_PRO) { if (wave >= 6) lds_signal(&flags[F_Q], lane); }
      else if (wave >= 6) {
        const int src = WS_QG + (kvh * 8 + b) * 2048 + (wave - 6) * 1024 + lane * 16;
        v4u qp;
#pragma unroll 1
        for (unsigned it = 0; it < SPIN_G; ++it) {
          CBAR();
          qp = bld<AUX_SC1>(rws, src, 0);
          if (__all(tag_ok(qp, tag))) break;
          __builtin_amdgcn_s_sleep(4);
        }
        *(LAS v2u*)(L + L_Q + (wave - 6) * 512 + lane * 8) = v2u{qp.x, qp.z};
        lds_signal(&flags[F_Q], lane);
        STAMP(3);
      }
      __builtin_amdgcn_s_setprio(0);
    }
    lds_wait_ge<1>(&flags[F_Q], 2u);
    {
      v4u qf[4];
#pragma unroll
      for (int t = 0; t < 4; ++t) qf[t] = *(LAS v4u*)(L + L_Q + (c & 3) * 256 + t * 64 + g * 16);
      f32x4 S[4];
      const int kr = c * 256 + ((g ^ c) << 4);
#pragma unroll
      for (int tau = 0; tau < 4; ++tau) {
        S[tau] = f32x4{0.f, 0.f, 0.f, 0.f};
#pragma unroll
        for (int t = 0; t < 4; ++t) S[tau] = mfma(qf[t], *(LAS v4u*)(LK + tau * 4096 + (kr ^ (t * 64))), S[tau]);
      }
      float sc[4];
#pragma unroll
      for (int r = 0; r < 4; ++r) {
        const float sa = (lane & 16) ? S[1][r] : S[0][r], sb = (lane & 16) ? S[3][r] : S[2][r];
        sc[r] = (lane & 32) ? sb : sa;
      }
      const unsigned r01 = pk_bf16(sc[0], sc[1]), r23 = pk_bf16(sc[2], sc[3]);
      const unsigned s01 = pk_bf16(bflo(r01) * 0.08838834764831845f, bfhi(r01) * 0.08838834764831845f);
      const unsigned s23 = pk_bf16(bflo(r23) * 0.08838834764831845f, bfhi(r23) * 0.08838834764831845f);
      sc[0] = bflo(s01); sc[1] = bfhi(s01); sc[2] = bflo(s23); sc[3] = bfhi(s23);
      auto p02 = __builtin_amdgcn_permlane32_swap(__float_as_uint(sc[0]), __float_as_uint(sc[2]), false, false);
      auto p13 = __builtin_amdgcn_permlane32_swap(__float_as_uint(sc[1]), __float_as_uint(sc[3]), false, false);
      const float m02 = fmax_raw(__uint_as_float(p02[0]), __uint_as_float(p02[1]));
      const float m13 = fmax_raw(__uint_as_float(p13[0]), __uint_as_float(p13[1]));
      auto pq = __builtin_amdgcn_permlane16_swap(__float_as_uint(m02), __float_as_uint(m13), false, false);
      float m = fmax_raw(__uint_as_float(pq[0]), __uint_as_float(pq[1]));
      m = fmax_raw(m, dppf<XOR1>(m)); m = fmax_raw(m, dppf<XOR2>(m)); m = fmax_raw(m, dppf<HMIR>(m)); m = fmax_raw(m, dppf<ROR8>(m));
      m *= 1.4426950408889634f;
      LAS u16* pw = (LAS u16*)(LK + LK_P);
      LGKM0();
#pragma unroll
      for (int r = 0; r < 4; ++r) {
        const float mr = __uint_as_float(__builtin_amdgcn_readlane(__float_as_uint(m), 16 * r));
        const float p = __builtin_amdgcn_exp2f(fmaf(sc[r], 1.4426950408889634f, -mr));
        pw[r * 64 + lane] = (u16)pk_bf16(p, p);
      }
      v4u pf[2];
#pragma unroll
      for (int u = 0; u < 2; ++u) pf[u] = *(LAS v4u*)(LK + LK_P + (c >> 2) * 128 + u * 64 + g * 16);
      const v4u ones = {0x3f803f80u, 0x3f803f80u, 0x3f803f80u, 0x3f803f80u};
      f32x4 ls = mfma(pf[0], ones, f32x4{0.f, 0.f, 0.f, 0.f});
      ls = mfma(pf[1], ones, ls);
      if (c == 0) {
        *(LAS float*)(L + L_ML + g * 32 + wave * 4) = m;
        *(LAS float*)(L + L_ML + 128 + g * 32 + wave * 4) = ls[0];
      }
      lds_signal(&flags[F_SOFT], lane);
      STAMP(5);
      if (wave >= 4) V_LOADS();
      f32x4 O[8];
      v4u Bt[8];
#define TRP(u, kp) _Pragma("unroll") for (int jp = 0; jp < 4; ++jp) { \
        const unsigned a_ = Vf[u][2 * (kp)][jp], b_ = Vf[u][2 * (kp) + 1][jp]; \
        Bt[2 * jp][kp] = __builtin_amdgcn_perm(b_, a_, 0x05040100u); Bt[2 * jp + 1][kp] = __builtin_amdgcn_perm(b_, a_, 0x07060302u); }
      TRP(0, 0) TRP(0, 1) TRP(0, 2) TRP(0, 3)
#pragma unroll
      for (int j = 0; j < 8; ++j) O[j] = mfma(pf[0], Bt[j], f32x4{0.f, 0.f, 0.f, 0.f});
      SCHED();
      TRP(1, 0) TRP(1, 1) TRP(1, 2)
#pragma unroll
      for (int j = 0; j < 8; ++j) { PIN(Bt[j]); PIN(O[j]); }
      SCHED();
      VMCNT(HEADS_UP_VM);
      lds_signal(&flags[F_NEAR], lane);
      SCHED();
      TRP(1, 3)
#pragma unroll
      for (int j = 0; j < 8; ++j) O[j] = mfma(pf[1], Bt[j], O[j]);
      LAS unsigned char* ap = LK + LK_ACC + g * 512 + c * 32;
      *(LAS f32x4*)(ap) = f32x4{O[0][0], O[1][0], O[2][0], O[3][0]};
      *(LAS f32x4*)(ap + 16) = f32x4{O[4][0], O[5][0], O[6][0], O[7][0]};
      STAMP(6);
    }

    f32x4 D;
    v4u Wa[4];
    if (NO_TAIL) { wg_barrier(); STAMP(7); } else {
    if (wave < 4) {
      lds_wait_ge<1>(&flags[F_NEAR], 8u);
      const v4i rwo = make_srd(wo + (size_t)h2 * 128 * 512, 128 * 1024);
      const v4i rw2 = make_srd(w2 + (size_t)h2 * 56 * 512, 56 * 1024);
      const int wv = (lane >> 3) * 1024 + cb8 * 128 + (lane & 7) * 16;
      const unsigned lw = (unsigned)(size_t)(L + L_W);
#pragma unroll
      for (int i = 0; i < 4; ++i) dma1(rwo, wv + (wave * 4 + i) * 8192, lw + (wave * 4 + i) * 1024);
#pragma unroll
      for (int i = 0; i < 2; ++i) dma1(rw2, wv + (wave * 2 + i) * 8192, lw + 16384 + (wave * 2 + i) * 1024);
      wg_barrier();
      STAMP(7);
      VMCNT(0);
      lds_signal(&flags[F_W], lane);
      lds_wait_ge<0>(&flags[F_W], 4u);
      LAS unsigned char* tb = L + L_W + (8 * g + (c >> 2)) * 128 + wave * 32 + (c & 3) * 8;
#pragma unroll
      for (int ks = 0; ks < 4; ++ks) Wa[ks] = tr_frag(tb + ks * 4096, 512);
      D = mfma(tr_frag(tb + 4 * 4096, 512), *(LAS v4u*)(L + L_H + (c & 7) * 144 + g * 16), f32x4{0.f, 0.f, 0.f, 0.f});
      D = mfma(tr_frag(tb + 5 * 4096, 512), *(LAS v4u*)(L + L_H + (c & 7) * 144 + 64 + g * 16), D);
#pragma unroll
      for (int ks = 0; ks < 4; ++ks) PIN(Wa[ks]);
      PIN(D);
      STAMP(8);
    } else {
      wg_barrier();
      STAMP(7);
      const int hh = wave - 4;
      const f32x4 w0 = *(LAS f32x4*)(L + L_WT + hh * 32), w1v = *(LAS f32x4*)(L + L_WT + hh * 32 + 16);
      f32x2 a[8];
#pragma unroll
      for (int w = 0; w < 8; ++w) a[w] = *(LAS f32x2*)(L + L_K + w * 16384 + LK_ACC + hh * 512 + lane * 8);
      float o0 = 0.f, o1 = 0.f;
#pragma unroll
      for (int w = 0; w < 8; ++w) {
        const float wt = w < 4 ? w0[w & 3] : w1v[w & 3];
        o0 = fmaf(wt, a[w][0], o0); o1 = fmaf(wt, a[w][1], o1);
      }
      const unsigned pk = pk_bf16(o0, o1);
      const unsigned nb = dppu<XOR1>(pk);
      const int dst = WS_H1 + (((kvh * 4 + hh) * 8 + b) * 4 + s) * 512 + (lane >> 1) * 16;
      if (!(lane & 1)) bst_sc1(v4u{pk, tag, nb, tag}, rws, dst);
      STAMP(8);
    }
    {
      const int srcl = WS_LSE + ((kvh * 8 + wave) * 4 + (lane & 3)) * 128 + (h2 & 2) * 8;
      v4u ls;
#pragma unroll 1
      for (unsigned it = 0; it < SPIN_G; ++it) {
        CBAR();
        ls = bld<AUX_SC1>(rws, srcl, 0);
        if (__all(tag_ok(ls, tag))) break;
      }
      STAMP(9);
      const int src = WS_H1 + (h2 * 8 + wave) * 2048 + lane * 16;
      v4u u0 = bld<AUX_SC1>(rws, src, 0), u1 = bld<AUX_SC1>(rws, src + 1024, 0);
      SCHED();
      const float e = __uint_as_float((h2 & 1) ? ls.z : ls.x);
      float M = fmax_raw(e, dppf<XOR1>(e)); M = fmax_raw(M, dppf<XOR2>(M));
      float w = __builtin_amdgcn_exp2f(e - M);
      float Ls = w + dppf<XOR1>(w); Ls += dppf<XOR2>(Ls);
      w *= __builtin_amdgcn_rcpf(Ls);
      const float wa = (lane & 32) ? dppf<0x55>(w) : dppf<0x00>(w), wb = (lane & 32) ? dppf<0xFF>(w) : dppf<0xAA>(w);
#pragma unroll 1
      for (unsigned it = 0; it < SPIN_G; ++it) {
        if (__all(tag_ok(u0, tag) & tag_ok(u1, tag))) break;
        CBAR();
        u0 = bld<AUX_SC1>(rws, src, 0); u1 = bld<AUX_SC1>(rws, src + 1024, 0);
      }
      const float o0 = x32_sum(fmaf(wa, bflo(u0.x), wb * bflo(u1.x))), o1 = x32_sum(fmaf(wa, bfhi(u0.x), wb * bfhi(u1.x)));
      const float o2 = x32_sum(fmaf(wa, bflo(u0.z), wb * bflo(u1.z))), o3 = x32_sum(fmaf(wa, bfhi(u0.z), wb * bfhi(u1.z)));
      if (lane < 32) *(LAS v2u*)(L + L_O + wave * 272 + lane * 8) = v2u{pk_bf16(o0, o1), pk_bf16(o2, o3)};
      lds_signal(&flags[F_O], lane);
      STAMP(10);
    }
    if (wave < 4) {
      lds_wait_ge<0>(&flags[F_O], 8u);
#pragma unroll
      for (int ks = 0; ks < 4; ++ks) D = mfma(Wa[ks], *(LAS v4u*)(L + L_O + (c & 7) * 272 + ks * 64 + g * 16), D);
      const int dst = WS_H2 + ((cb8 * 16 + wave * 4 + g) * 32 + h2) * 128 + c * 16;
      if (c < 8) bst_sc1(v4u{pk_bf16(D[0], D[1]), tag, pk_bf16(D[2], D[3]), tag}, rws, dst);
      STAMP(2);
    }
    }
  }
}

extern "C" void kernel_launch(void* const* d_in, const int* in_sizes, int n_in, void* d_out, int out_size, void* d_ws,
                              size_t ws_size, hipStream_t stream) {
  fused_kernel<<<256, NWAVE * 64, 0, stream>>>((const u16*)d_in[0], (const u16*)d_in[1], (const u16*)d_in[2], (const u16*)d_in[3],
                                               (const u16*)d_in[4], (const u16*)d_in[6], (const u16*)d_in[7], (u16*)d_out,
                                               (unsigned char*)d_ws);
}
```

```cpp
#include <hip/hip_runtime.h>
#include <stdint.h>

typedef unsigned short u16;
typedef unsigned v4u __attribute__((ext_vector_type(4)));
typedef unsigned v2u __attribute__((ext_vector_type(2)));
typedef float f32x4 __attribute__((ext_vector_type(4)));
typedef float f32x2 __attribute__((ext_vector_type(2)));
typedef __bf16 bf16x8 __attribute__((ext_vector_type(8)));
typedef __bf16 bf16x2 __attribute__((ext_vector_type(2)));
typedef short v4i16 __attribute__((ext_vector_type(4)));
typedef int v4i __attribute__((ext_vector_type(4)));
#define LAS __attribute__((address_space(3)))

#define WIN 32
#define WIN2 48
#define WE 3
#define PACE_C 924
#define PACE_D 29
#define KD 29
#define KEND 490
#define QDELAY 295
#define WDELAY 122
#define FDELAY 44
#define WSTART 50
#define WSTEP 10
#define HDELAY 12
#define TRY6 5
#define CODE_BYTES 15100

static_assert(WE >= 2 && WE <= 6, "w2 is needed early");
constexpr int NWAVE = 9;
constexpr int AUX_NT = 2, AUX_SC1 = 16, AUX_SC1_NT = 18;
constexpr int RSRC_W3 = 0x00020000;
constexpr int OOB = 0x40000000;
constexpr int WS_EPOCH = 0;
constexpr int WS_T0 = 4096;
constexpr int WS_QG = 8192;
constexpr int WS_HG = WS_QG + 8 * 8 * 4 * 128 * 16;
constexpr int WS_ML = WS_HG + 28 * 4 * 8 * 16 * 16;
constexpr int WS_H1 = WS_ML + 256 * 128;
constexpr int WS_H2 = WS_H1 + 32 * 8 * 4 * 512;
constexpr int WS_END = WS_H2 + 128 * 32 * 128;
constexpr int L_K = 0;
constexpr int LK_ACC = 0, LK_P = 2048;
constexpr int L_PW = L_K + 2 * 32768;
constexpr int L_W = 131072;
constexpr int L_XN = L_W;
constexpr int L_Q = L_W + 24576;
constexpr int L_O = L_Q + 1024;
constexpr int L_H = L_O + 8 * 272;
constexpr int L_ML = L_H + 8 * 144;
constexpr int L_WT = L_ML + 256;
constexpr int L_FLAG = L_WT + 128;
constexpr int L_END = L_FLAG + 128;
enum { F_X, F_T, F_L, F_P, F_Q, F_G, F_V, F_ISSUE, F_SOFT, F_WT, F_ACC, F_ACC6, F_NEAR, F_W, F_W2, F_O, F_N, F_TAG = 20, F_T0, F_TL };

constexpr unsigned SPIN_G = 1u << 18;
constexpr unsigned SPIN_L = 1u << 25;
constexpr float LOG2E = 1.4426950408889634f;

__device__ __forceinline__ float bflo(unsigned v) { return __uint_as_float(v << 16); }
__device__ __forceinline__ float bfhi(unsigned v) { return __uint_as_float(v & 0xffff0000u); }
__device__ __forceinline__ unsigned pk_bf16(float lo, float hi) {
  f32x2 f = {lo, hi};
  return __builtin_bit_cast(unsigned, __builtin_convertvector(f, bf16x2));
}
__device__ __forceinline__ float rbf(float x) { return bflo(pk_bf16(x, x)); }
__device__ __forceinline__ f32x4 mfma(v4u a, v4u b, f32x4 c) {
  return __builtin_amdgcn_mfma_f32_16x16x32_bf16(__builtin_bit_cast(bf16x8, a), __builtin_bit_cast(bf16x8, b), c, 0, 0, 0);
}
template <int CTRL> __device__ __forceinline__ float dppf(float x) {
  return __builtin_bit_cast(float, __builtin_amdgcn_mov_dpp(__builtin_bit_cast(int, x), CTRL, 0xf, 0xf, true));
}
template <int CTRL> __device__ __forceinline__ int dppi(int x) { return __builtin_amdgcn_mov_dpp(x, CTRL, 0xf, 0xf, true); }
constexpr int XOR1 = 0xB1, XOR2 = 0x4E, HMIR = 0x141, ROR8 = 0x128;
__device__ __forceinline__ float fmax_raw(float a, float b) { return __builtin_amdgcn_fmed3f(a, b, __builtin_inff()); }
__device__ __forceinline__ float rdlane(float v, int l) { return __uint_as_float(__builtin_amdgcn_readlane(__float_as_uint(v), l)); }
__device__ __forceinline__ float fold32(float a, float b) {
  auto t = __builtin_amdgcn_permlane32_swap(__float_as_uint(a), __float_as_uint(b), false, false);
  return __uint_as_float(t[0]) + __uint_as_float(t[1]);
}
__device__ __forceinline__ float sum16(float x) {
  auto s = __builtin_amdgcn_permlane16_swap(__float_as_uint(x), __float_as_uint(x), false, false);
  return __uint_as_float(s[0]) + __uint_as_float(s[1]);
}
template <int AUX> __device__ __forceinline__ v4u bld(__amdgpu_buffer_rsrc_t r, int voff, int soff) {
  return __builtin_amdgcn_raw_buffer_load_b128(r, voff, soff, AUX);
}
template <int AUX> __device__ __forceinline__ void bst(v4u v, __amdgpu_buffer_rsrc_t r, int voff) {
  __builtin_amdgcn_raw_buffer_store_b128(v, r, voff, 0, AUX);
}
__device__ __forceinline__ bool tag_ok(v4u v, unsigned tag) { return (v.y == tag) & (v.w == tag); }
__device__ __forceinline__ bool all4_ok(const v4u (&u)[4], unsigned tag) {
  const unsigned lo = (u[0].y & u[0].w & u[1].y) & (u[1].w & u[2].y & u[2].w) & (u[3].y & u[3].w & tag);
  const unsigned hi = (u[0].y | u[0].w | u[1].y) | (u[1].w | u[2].y | u[2].w) | (u[3].y | u[3].w | tag);
  return __builtin_amdgcn_ballot_w64(lo != hi) == 0ull;
}
__device__ __forceinline__ v4u pack_f26(f32x4 v, unsigned tag) {
  const unsigned t0 = tag & 63u, t1 = (tag >> 6) & 63u;
  return v4u{((__float_as_uint(v[0]) + 32u) & ~63u) | t0, ((__float_as_uint(v[1]) + 32u) & ~63u) | t1,
             ((__float_as_uint(v[2]) + 32u) & ~63u) | t0, ((__float_as_uint(v[3]) + 32u) & ~63u) | t1};
}
__device__ __forceinline__ f32x4 unpack_f26(v4u u) {
  return f32x4{__uint_as_float(u.x & ~63u), __uint_as_float(u.y & ~63u), __uint_as_float(u.z & ~63u), __uint_as_float(u.w & ~63u)};
}
__device__ __forceinline__ bool tag12_ok(v4u v, unsigned tag) { return ((((v.x ^ tag) | (v.z ^ tag)) | ((v.y ^ (tag >> 6)) | (v.w ^ (tag >> 6)))) & 63u) == 0u; }
#define CBAR() asm volatile("" ::: "memory")
#define VMCNT(n) asm volatile("s_waitcnt vmcnt(%0)" ::"i"(n) : "memory")
#define SCHED() __builtin_amdgcn_sched_barrier(0)
#define PIN(x) asm volatile("" : "+v"(x))
__device__ __forceinline__ unsigned lds_get(LAS unsigned* p) { return __builtin_amdgcn_readfirstlane(*(volatile LAS unsigned*)p); }
template <int SLEEP> __device__ __forceinline__ void lds_wait_ge(LAS unsigned* p, unsigned want) {
#pragma unroll 1
  for (unsigned i = 0; i < SPIN_L; ++i) {
    if (lds_get(p) >= want) break;
    if (SLEEP) __builtin_amdgcn_s_sleep(SLEEP);
  }
  CBAR();
}
__device__ __forceinline__ void lds_signal(LAS unsigned* p, unsigned n = 1u) {
  unsigned long long keep;
  asm volatile("s_mov_b64 %0, exec\n\ts_mov_b64 exec, 1\n\tds_add_u32 %1, %2\n\ts_mov_b64 exec, %0" : "=&s"(keep) : "v"((unsigned)(size_t)p), "v"(n) : "memory");
}
__device__ __forceinline__ void lds_set(LAS unsigned* p, unsigned v) {
  asm volatile("ds_write_b32 %0, %1" ::"v"((unsigned)(size_t)p), "v"(v) : "memory");
}
__device__ __forceinline__ unsigned clk32() {
  unsigned long long t;
  asm volatile("s_memrealtime %0\n\ts_waitcnt lgkmcnt(0)" : "=s"(t)::"memory");
  return (unsigned)t;
}
__device__ __forceinline__ void wait_until(unsigned t) {
#pragma unroll 1
  for (int i = 0; i < 1024 && (int)(clk32() - t) < 0; ++i) __builtin_amdgcn_s_sleep(1);
}
__device__ __forceinline__ void spin_until(unsigned t) {
  int lim = -2048;
  asm volatile("1:\n\ts_memrealtime vcc\n\ts_add_u32 %0, %0, 1\n\ts_waitcnt lgkmcnt(0)\n\ts_sub_u32 vcc_lo, vcc_lo, %1\n\t"
               "s_and_b32 vcc_lo, vcc_lo, %0\n\ts_cmp_lt_i32 vcc_lo, 0\n\ts_cbranch_scc1 1b"
               : "+s"(lim) : "s"(t) : "memory", "scc", "vcc");
}
__device__ __forceinline__ void lds_spin_ge(LAS unsigned* p, unsigned want) {
  int lim = -(int)SPIN_L; unsigned v, sv;
  asm volatile("1:\n\tds_read_b32 %1, %3\n\ts_add_u32 %0, %0, 1\n\ts_waitcnt lgkmcnt(0)\n\tv_readfirstlane_b32 %2, %1\n\ts_sub_u32 %2, %2, %4\n\t"
               "s_and_b32 %2, %2, %0\n\ts_cmp_lt_i32 %2, 0\n\ts_cbranch_scc1 1b"
               : "+s"(lim), "=&v"(v), "=&s"(sv) : "v"((unsigned)(size_t)p), "s"(want) : "memory", "scc");
}
__device__ __forceinline__ v4i make_srd(const void* p, int bytes) {
  const unsigned long long a = (unsigned long long)p;
  return v4i{(int)(unsigned)a, (int)(unsigned)(a >> 32), bytes, RSRC_W3};
}
__device__ __forceinline__ void dma4(v4i srd, int v0, int v1, int v2, int v3, int soff, unsigned lds) {
  unsigned keep;
  asm volatile(
      "s_mov_b32 %0, m0\n\ts_mov_b32 m0, %7\n\ts_nop 0\n\t"
      "buffer_load_dwordx4 %1, %5, %6 offen nt lds\n\t"
      "buffer_load_dwordx4 %2, %5, %6 offen offset:1024 nt lds\n\t"
      "buffer_load_dwordx4 %3, %5, %6 offen offset:2048 nt lds\n\t"
      "buffer_load_dwordx4 %4, %5, %6 offen offset:3072 nt lds\n\t"
      "s_mov_b32 m0, %0"
      : "=&s"(keep) : "v"(v0), "v"(v1), "v"(v2), "v"(v3), "s"(srd), "s"(soff), "s"(lds) : "memory");
}
template <int W> __device__ __forceinline__ void k_run4(v4i srd, int v0, int v1, int v2, int v3, int blk, int til, unsigned lds, unsigned& t2, int d) {
  unsigned keep; int n = 4, lim = 2000, so, so2;
  asm volatile(
      "s_mov_b32 %0, m0\n\ts_mov_b32 m0, %13\n"
      "1:\n\ts_memrealtime vcc\n\ts_and_b32 %6, %1, 0x70000\n\ts_and_b32 %7, %2, 0x3000\n\ts_or_b32 %6, %6, %7\n\t"
      "s_sub_u32 %5, %5, 1\n\ts_cmp_lt_i32 %5, 0\n\ts_waitcnt lgkmcnt(0)\n\ts_cbranch_scc1 3f\n\t"
      "s_lshl_b32 vcc_lo, vcc_lo, 1\n\ts_sub_u32 vcc_lo, vcc_lo, %4\n\ts_cmp_lt_i32 vcc_lo, 0\n\ts_cbranch_scc1 1b\n"
      "3:\n\ts_waitcnt vmcnt(%15)\n\t"
      "buffer_load_dwordx4 %8, %12, %6 offen nt lds\n\t"
      "buffer_load_dwordx4 %9, %12, %6 offen offset:1024 nt lds\n\t"
      "buffer_load_dwordx4 %10, %12, %6 offen offset:2048 nt lds\n\t"
      "buffer_load_dwordx4 %11, %12, %6 offen offset:3072 nt lds\n\t"
      "s_add_u32 m0, m0, 0x1000\n\ts_add_u32 %1, %1, 0x10000\n\ts_add_u32 %2, %2, 0x1000\n\ts_add_u32 %4, %4, %14\n\t"
      "s_sub_u32 %3, %3, 1\n\ts_cmp_gt_i32 %3, 0\n\ts_cbranch_scc1 1b\n\t"
      "s_mov_b32 m0, %0"
      : "=&s"(keep), "+s"(blk), "+s"(til), "+s"(n), "+s"(t2), "+s"(lim), "=&s"(so), "=&s"(so2)
      : "v"(v0), "v"(v1), "v"(v2), "v"(v3), "s"(srd), "s"(lds), "s"(d), "i"(W - 4) : "memory", "scc", "vcc");
}
__device__ __forceinline__ v4u tr_frag(LAS unsigned char* p, int rowstep4) {
  v2u a = __builtin_bit_cast(v2u, __builtin_amdgcn_ds_read_tr16_b64_v4i16((LAS v4i16*)p));
  v2u b = __builtin_bit_cast(v2u, __builtin_amdgcn_ds_read_tr16_b64_v4i16((LAS v4i16*)(p + rowstep4)));
  return v4u{a.x, a.y, b.x, b.y};
}

__global__ void __launch_bounds__(NWAVE * 64, 3)
fused_kernel(const u16* __restrict__ x, const u16* __restrict__ kc, const u16* __restrict__ vc, const u16* __restrict__ wq,
             const u16* __restrict__ wo, const u16* __restrict__ w1, const u16* __restrict__ w2, u16* __restrict__ out,
             unsigned char* __restrict__ ws) {
  __shared__ __attribute__((aligned(16))) unsigned char smem[L_END];
  LAS unsigned char* L = (LAS unsigned char*)smem;
  unsigned cpf;
  {
    unsigned long long pc0;
    asm volatile("s_getpc_b64 %0" : "=s"(pc0));
    asm volatile("global_load_dword %0, %1, %2" : "=&v"(cpf) : "v"(min((int)(threadIdx.x >> 2) * 128, CODE_BYTES)), "s"(pc0) : "memory");
  }
  const int wg = blockIdx.x, kvh = wg & 7, idx = wg >> 3, b = idx & 7, s = idx >> 3;
  const int lane = threadIdx.x & 63, wave = __builtin_amdgcn_readfirstlane(threadIdx.x >> 6) - 1;
  const unsigned l0 = (unsigned)(size_t)(L + L_K);
  LAS unsigned* flags = (LAS unsigned*)(L + L_FLAG);
  v4u xr;
  unsigned ep = 0;
  {
    int q0 = (s * 128 + (lane >> 3)) * 8192 + (kvh * 8 + b) * 128 + (lane & 7) * 16, q1 = q0 + 64512, q2 = q0 + 2 * 64512, q3 = q0 + 3 * 64512;
    int xo = (int)threadIdx.x * 16 - 1024;
    asm volatile("" : "+v"(q0), "+v"(q1), "+v"(q2), "+v"(q3), "+v"(xo));
    SCHED();
    asm volatile("" ::"s"(x), "s"(kc), "s"(vc), "s"(wq), "s"(wo), "s"(w1), "s"(w2), "s"(out), "s"(ws));
    if (wave < 0) {
      const v4i rw = make_srd(wq, 512 * 8192);
#pragma unroll 1
      for (int j = 0; j < 4; ++j) dma4(rw, q0, q1, q2, q3, j * 32 * 8192, l0 + L_PW + j * 4096);
      asm volatile("global_load_dword %0, %1, %2 sc1" : "=&v"(ep) : "v"(WS_EPOCH), "s"(ws) : "memory");
      flags[lane & 31] = 0u;
      xo = ((wg >> 1) & 7) * 1024 + (wg >> 4) * 64 + (lane & 6) * 8;
    }
    asm volatile("global_load_dwordx4 %0, %1, %2" : "=&v"(xr) : "v"(xo), "s"(x) : "memory");
  }
  SCHED();
  constexpr int pace_c = PACE_C, pace_d = PACE_D, fdelay = FDELAY, qdelay = QDELAY, wdelay = WDELAY, wstep = WSTEP, hdelay = HDELAY, wstart = WSTART, kd = KD, kend = KEND;
  const int c = lane & 15, g = lane >> 4;
  const unsigned kvoff = ((b * 8 + kvh) * 8192 + 6144 + s * 64) * 256;
  const int kvlen = 524288 - s * 16384;
  unsigned hsh = (unsigned)(wg + 1) * 0x9E3779B1u; hsh ^= hsh >> 15; hsh *= 0x85EBCA77u; hsh ^= hsh >> 13;
  const int hw = (int)((hsh >> 8) & 3u);
  const int k0 = g * 256 + ((c ^ g) << 4), k1 = k0 ^ 64, k2 = k0 ^ 128, k3 = k0 ^ 192;
  const v4i rk = make_srd((const unsigned char*)kc + kvoff, kvlen);
#define K_TILE(w, tau) dma4(rk, k0, k1, k2, k3, (((w) + b) & 7) * 65536 + (((tau) + hw + (w)) & 3) * 4096, l0 + (tau) * 32768 + (w) * 4096)
  const int kw = (wave + 2) & 7;
  asm volatile("" ::"s"(rk), "v"(k0), "v"(k1), "v"(k2), "v"(k3));
  const unsigned t_bar = clk32();
  __builtin_amdgcn_s_barrier(); CBAR();
  if ((wave & 3) != 3) K_TILE(kw, 0);
  SCHED();

  const __amdgpu_buffer_rsrc_t rws = __builtin_amdgcn_make_buffer_rsrc((void*)ws, 0, WS_END, RSRC_W3);
  const int h2 = kvh * 4 + (idx & 3), cb8 = idx >> 2;
  const bool has_f = (h2 & 7) != 7;
  const int fb = h2 - (h2 >> 3);
  const bool has_h = idx < 14;
  const int hu = 14 * kvh + idx;

  const int wrot = (h2 + kvh) & 3;
#define WGRP(j) (((j) + 4) % 6)
#define W_LOADS(j0, j1, HOOK) do { \
    const v4i rwo = make_srd(wo + (size_t)h2 * 128 * 512, 128 * 1024), rw2 = make_srd(w2 + (size_t)(has_f ? fb : 0) * 64 * 512, has_f ? 64 * 1024 : 0); \
    const int wv = (lane >> 3) * 1024 + cb8 * 128 + (lane & 7) * 16; \
    _Pragma("unroll") for (int j = (j0); j < (j1); ++j) { HOOK; \
      dma4(WGRP(j) < 4 ? rwo : rw2, wv, wv + 7168, wv + 2 * 7168, wv + 3 * 7168, (WGRP(j) < 4 ? WGRP(j) ^ wrot : (WGRP(j) ^ fb) & 1) * 32768, l0 + L_W + WGRP(j) * 4096); } } while (0)

  if (__builtin_expect(wave < 0, 1)) {
    const int set = wg >> 1;
    const int ooff = (set & 7) * 1024 + (set >> 3) * 64 + (lane & 7) * 8;
    if (has_h) {
      const v4i rw1 = make_srd(w1, 512 * 3584);
      const int u0 = ((hu & 3) * 128 + (lane >> 3)) * 3584 + (hu >> 2) * 128 + (lane & 7) * 16;
#pragma unroll 1
      for (int j = 0; j < 4; ++j) dma4(rw1, u0, u0 + 27648, u0 + 2 * 27648, u0 + 3 * 27648, j * 32 * 3584, l0 + L_PW + 32768 + j * 4096);
    } else {
#pragma unroll 1
      for (int j = 0; j < 4; ++j) K_TILE(4 + j, 1);
    }
    VMCNT(16);
    PIN(ep); PIN(xr); PIN(cpf);
    const unsigned tag = __builtin_amdgcn_readfirstlane(ep) + 1u;
    lds_set(&flags[F_TAG], tag);
    lds_set(&flags[F_T0], clk32());
    lds_set(&flags[F_T], 1u);
    unsigned t2 = 2u * (t_bar + kend) - (unsigned)((has_h ? 24 : 20) * kd);
#pragma unroll 1
    for (int r = has_h ? 0 : 1; r < 6; ++r) {
      const int w0 = r < 3 ? 4 : 0, tau = r < 3 ? 1 + r : r - 2;
      if (r == 4) lds_wait_ge<1>(&flags[F_P], has_h ? 2u : 1u);
      if (!has_h || r < 2) k_run4<WIN>(rk, k0, k1, k2, k3, (w0 + b) * 65536, (tau + hw + w0) * 4096, l0 + tau * 32768 + w0 * 4096, t2, kd);
      else k_run4<WIN2>(rk, k0, k1, k2, k3, (w0 + b) * 65536, (tau + hw + w0) * 4096, l0 + tau * 32768 + w0 * 4096, t2, kd);
      if (r == 1 || r == 4) { VMCNT(32); lds_set(&flags[F_L], (unsigned)r); }
    }
    VMCNT(WIN2 - 4 * WE);
    W_LOADS(0, WE, (void)0);
    lds_set(&flags[F_V], 1u);
    VMCNT(4 * WE);
    lds_set(&flags[F_L], 8u);
    VMCNT(0);
    lds_signal(&flags[F_W]);
    lds_wait_ge<2>(&flags[F_SOFT], 8u);
    const float m = *(LAS float*)(L + L_ML + (lane & 31) * 4), l = *(LAS float*)(L + L_ML + 128 + (lane & 31) * 4);
    float M = fmax_raw(m, dppf<XOR1>(m)); M = fmax_raw(M, dppf<XOR2>(M)); M = fmax_raw(M, dppf<HMIR>(M));
    const float e = __builtin_amdgcn_exp2f((m - M) * LOG2E);
    float Ls = e * l; Ls += dppf<XOR1>(Ls); Ls += dppf<XOR2>(Ls); Ls += dppf<HMIR>(Ls);
    *(LAS float*)(L + L_WT + (lane & 31) * 4) = e * __builtin_amdgcn_rcpf(Ls);
    lds_set(&flags[F_WT], 1u);
    v4u lv = v4u{__float_as_uint(M), tag, __float_as_uint(Ls), tag};
    int ldst = (lane & 39) == 0 ? WS_ML + ((kvh * 8 + b) * 4 + s) * 128 + lane * 2 : OOB;
    int fsrc = WS_H2 + set * 4096 + lane * 16, odst = (lane & 24) == 0 ? ooff + (lane >> 5) * 4 : OOB;
    const unsigned xr2 = (lane & 1) ? ((lane & 32) ? xr.w : xr.z) : ((lane & 32) ? xr.y : xr.x);
    int edst = (wg == 0 && lane == 0) ? WS_EPOCH : OOB;
    float xa = bflo(xr2), xb2 = bfhi(xr2);
    PIN(lv); PIN(ldst); PIN(fsrc); PIN(odst); PIN(edst); PIN(xa); PIN(xb2);
    lds_wait_ge<2>(&flags[F_ACC6], 2u);
    __builtin_amdgcn_s_setprio(3);
    lds_spin_ge(&flags[F_NEAR], 1u);
    bst<AUX_SC1>(lv, rws, ldst);
    if (!(wg & 1)) {
      const __amdgpu_buffer_rsrc_t ro = __builtin_amdgcn_make_buffer_rsrc((void*)out, 0, 8192, RSRC_W3);
      VMCNT(0);
      lds_spin_ge(&flags[F_O], 4u);
      spin_until(clk32() + fdelay);
      v4u u[4];
#pragma unroll
      for (int n = 0; n < 4; ++n) u[n] = bld<AUX_SC1>(rws, fsrc + n * 1024, 0);
      if (__builtin_expect(!all4_ok(u, tag), 0)) {
        bool ok = false;
#pragma unroll 1
        for (unsigned it = 0; it < SPIN_G; ++it) {
          CBAR();
          if (!ok) {
#pragma unroll
            for (int n = 0; n < 4; ++n) u[n] = bld<AUX_SC1>(rws, fsrc + n * 1024, 0);
            ok = tag_ok(u[0], tag) & tag_ok(u[1], tag) & tag_ok(u[2], tag) & tag_ok(u[3], tag);
          }
          if (__all(ok)) break;
        }
      }
      const float a0 = (bflo(u[0].x) + bflo(u[1].x)) + (bflo(u[2].x) + bflo(u[3].x));
      const float a1 = (bfhi(u[0].x) + bfhi(u[1].x)) + (bfhi(u[2].x) + bfhi(u[3].x));
      const float a2 = (bflo(u[0].z) + bflo(u[1].z)) + (bflo(u[2].z) + bflo(u[3].z));
      const float a3 = (bfhi(u[0].z) + bfhi(u[1].z)) + (bfhi(u[2].z) + bfhi(u[3].z));
      float p0 = fold32(a0, a2), p1 = fold32(a1, a3);
      p0 += dppf<ROR8>(p0); p1 += dppf<ROR8>(p1);
      p0 = sum16(p0); p1 = sum16(p1);
      __builtin_amdgcn_raw_buffer_store_b32(pk_bf16(xa + p0, xb2 + p1), ro, odst, 0, 0);
      __builtin_amdgcn_raw_buffer_store_b32(tag, rws, edst, 0, AUX_SC1);
    }
  } else {
    LAS unsigned char* LK = L + L_K + kw * 4096;
    {
      if ((wave & 3) != 3) VMCNT(4); else VMCNT(0);
      PIN(xr); PIN(cpf);
      float f[8], d[8];
#pragma unroll
      for (int k = 0; k < 4; ++k) { f[2 * k] = bflo(xr[k]); f[2 * k + 1] = bfhi(xr[k]); }
      const float piv = rdlane(f[0], 0);
#pragma unroll
      for (int k = 0; k < 8; ++k) d[k] = f[k] - piv;
      const float s1 = ((d[0] + d[1]) + (d[2] + d[3])) + ((d[4] + d[5]) + (d[6] + d[7]));
      const float s2 = fmaf(d[0], d[0], fmaf(d[1], d[1], fmaf(d[2], d[2], d[3] * d[3]))) + fmaf(d[4], d[4], fmaf(d[5], d[5], fmaf(d[6], d[6], d[7] * d[7])));
      float t = fold32(s1, s2);
      t += dppf<XOR1>(t); t += dppf<XOR2>(t); t += dppf<HMIR>(t); t += dppf<ROR8>(t);
      const float a1 = (rdlane(t, 0) + rdlane(t, 16)) * (1.f / 512.f), a2 = (rdlane(t, 32) + rdlane(t, 48)) * (1.f / 512.f);
      const float mb = rbf(piv + a1), vb = rbf(fmaxf(fmaf(-a1, a1, a2), 0.f));
      const float rden = __builtin_amdgcn_rcpf(rbf(__builtin_amdgcn_sqrtf(rbf(vb + 1e-5f))));
      v4u xb;
#pragma unroll
      for (int k = 0; k < 4; ++k) {
        const unsigned dd = pk_bf16(f[2 * k] - mb, f[2 * k + 1] - mb);
        xb[k] = pk_bf16(bflo(dd) * rden, bfhi(dd) * rden);
      }
      *(LAS v4u*)(L + L_XN + wave * 1040 + lane * 16) = xb;
    }
    lds_signal(&flags[F_X]);
    const unsigned t_ln = clk32();
    if ((wave & 3) == 3) K_TILE(kw, 0);
    if (wave == 4 || (wave == 5 && has_h)) {
      const bool isq = wave == 4;
      const int kq = isq ? (idx >> 3) : (hu & 3);
      lds_spin_ge(&flags[F_X], 8u);
      v4u xb[4];
#pragma unroll
      for (int t = 0; t < 4; ++t) xb[t] = *(LAS v4u*)(L + L_XN + (c & 7) * 1040 + kq * 256 + t * 64 + g * 16);
      lds_spin_ge(&flags[isq ? F_T : F_L], 1u);
      const unsigned tg = lds_get(&flags[F_TAG]);
      LAS unsigned char* tb = L + L_PW + (wave - 4) * 32768 + (8 * g + (c >> 2)) * 128 + (c & 3) * 8;
      f32x4 z[4];
#pragma unroll
      for (int ct = 0; ct < 4; ++ct) z[ct] = f32x4{0.f, 0.f, 0.f, 0.f};
#pragma unroll
      for (int t = 0; t < 4; ++t)
#pragma unroll
        for (int ct = 0; ct < 4; ++ct) z[ct] = mfma(tr_frag(tb + ct * 32 + t * 4096, 512), xb[t], z[ct]);
      const int dst = (isq ? WS_QG + ((kvh * 8 + (c & 7)) * 4 + kq) * 2048 + (idx & 7) * 256 : WS_HG + (hu * 8 + (c & 7)) * 256) + ((c >> 3) * 4 + g) * 16;
      bst<AUX_SC1_NT>(pack_f26((c & 8) ? z[1] : z[0], tg), rws, dst);
      bst<AUX_SC1_NT>(pack_f26((c & 8) ? z[3] : z[2], tg), rws, dst + 128);
      lds_signal(&flags[F_P]);
    } else if (wave >= 6) {
      unsigned warm = __builtin_amdgcn_raw_buffer_load_b32(rws, WS_ML + kvh * 4096 + ((wave - 6) * 16 + c) * 128, 0, AUX_SC1);
      lds_wait_ge<1>(&flags[F_T], 1u);
      const unsigned tg = lds_get(&flags[F_TAG]);
      if (wave == 6 && wg < 32) {
        const unsigned ts = *(volatile LAS unsigned*)&flags[F_T0];
        bst<AUX_SC1>(v4u{ts, tg, ts, tg}, rws, lane == 0 ? WS_T0 + wg * 128 : OOB);
      }
      PIN(warm);
      wait_until(t_ln + qdelay);
      __builtin_amdgcn_s_setprio(2);
      const int src = WS_QG + (kvh * 8 + b) * 8192 + (wave - 6) * 1024 + lane * 16;
      const int tsrc = wave == 7 ? WS_T0 + (lane & 31) * 128 : OOB;
      const unsigned twant = wave == 7 ? tg : 0u;
      v4u qp[4], tv;
#pragma unroll 1
      for (unsigned it = 0; it < SPIN_G; ++it) {
        CBAR();
#pragma unroll
        for (int k = 0; k < 4; ++k) qp[k] = bld<AUX_SC1_NT>(rws, src, k * 2048);
        tv = bld<AUX_SC1>(rws, tsrc, 0);
        if (__all(tag12_ok(qp[0], tg) & tag12_ok(qp[1], tg) & tag12_ok(qp[2], tg) & tag12_ok(qp[3], tg) & tag_ok(tv, twant))) break;
      }
      __builtin_amdgcn_s_setprio(0);
      const f32x4 qv = (unpack_f26(qp[0]) + unpack_f26(qp[1])) + (unpack_f26(qp[2]) + unpack_f26(qp[3]));
      *(LAS v2u*)(L + L_Q + (wave - 6) * 512 + lane * 8) = v2u{pk_bf16(qv[0], qv[1]), pk_bf16(qv[2], qv[3])};
      if (wave == 7) {
        const unsigned base = __builtin_amdgcn_readfirstlane(tv.x);
        int dsum = (int)(tv.x - base);
        dsum += dppi<XOR1>(dsum); dsum += dppi<XOR2>(dsum); dsum += dppi<HMIR>(dsum); dsum += dppi<ROR8>(dsum);
        dsum = __builtin_amdgcn_readlane(dsum, 0) + __builtin_amdgcn_readlane(dsum, 16);
        lds_set(&flags[F_TL], base + (unsigned)((dsum >> 5) + pace_c));
        lds_set(&flags[F_G], 1u);
      }
      lds_signal(&flags[F_Q]);
    }
    if (wave == 2) {
      unsigned pt = __builtin_nontemporal_load((const __attribute__((address_space(1))) unsigned*)(wo + (size_t)h2 * 128 * 512 + cb8 * 64));
      PIN(pt);
    }
    VMCNT(0);
    lds_wait_ge<1>(&flags[F_T], 1u);
    const unsigned tag = lds_get(&flags[F_TAG]);

    const __amdgpu_buffer_rsrc_t rv = __builtin_amdgcn_make_buffer_rsrc((void*)((const unsigned char*)vc + kvoff), 0, kvlen, RSRC_W3);
    const int voff0 = ((8 * g + 16 * (hw + kw)) & 63) * 256 + c * 16, voff1 = voff0 ^ 8192, vso = ((b + kw) & 7) * 65536;
    v4u Vf[2][8];
#define V_LOADS() do { lds_wait_ge<1>(&flags[F_V], 1u); \
      const bool pace_ = lds_get(&flags[F_G]) != 0u; \
      const unsigned trel_ = lds_get(&flags[F_TL]) - (15 - 2 * wave) * pace_d; \
      if (pace_) wait_until(trel_); \
      _Pragma("unroll") for (int i = 0; i < 8; ++i) { Vf[0][i] = bld<AUX_NT>(rv, voff0 + i * 256, vso); if (i & 1) SCHED(); } \
      if (pace_) wait_until(trel_ + pace_d); \
      _Pragma("unroll") for (int i = 0; i < 8; ++i) { Vf[1][i] = bld<AUX_NT>(rv, voff1 + i * 256, vso); if (i & 1) SCHED(); } \
      SCHED(); lds_signal(&flags[F_ISSUE]); } while (0)
    if (wave < 2) V_LOADS();
    lds_wait_ge<1>(&flags[F_Q], 2u);
    lds_wait_ge<1>(&flags[F_L], kw < 4 ? 8u : 4u);
    v4u pf[2];
    {
      v4u qf[4];
#pragma unroll
      for (int t = 0; t < 4; ++t) qf[t] = *(LAS v4u*)(L + L_Q + (c & 3) * 256 + t * 64 + g * 16);
      f32x4 S[4];
      const int kr = c * 256 + ((g ^ c) << 4);
#pragma unroll
      for (int tau = 0; tau < 4; ++tau) {
        S[tau] = f32x4{0.f, 0.f, 0.f, 0.f};
#pragma unroll
        for (int t = 0; t < 4; ++t) S[tau] = mfma(qf[t], *(LAS v4u*)(LK + tau * 32768 + (kr ^ (t * 64))), S[tau]);
      }
      float sc[4];
#pragma unroll
      for (int r = 0; r < 4; ++r) {
        const float sa = (lane & 16) ? S[1][r] : S[0][r], sb = (lane & 16) ? S[3][r] : S[2][r];
        sc[r] = (lane & 32) ? sb : sa;
      }
      const unsigned r01 = pk_bf16(sc[0], sc[1]), r23 = pk_bf16(sc[2], sc[3]);
      const unsigned s01 = pk_bf16(bflo(r01) * 0.08838834764831845f, bfhi(r01) * 0.08838834764831845f);
      const unsigned s23 = pk_bf16(bflo(r23) * 0.08838834764831845f, bfhi(r23) * 0.08838834764831845f);
      sc[0] = bflo(s01); sc[1] = bfhi(s01); sc[2] = bflo(s23); sc[3] = bfhi(s23);
      auto p02 = __builtin_amdgcn_permlane32_swap(__float_as_uint(sc[0]), __float_as_uint(sc[2]), false, false);
      auto p13 = __builtin_amdgcn_permlane32_swap(__float_as_uint(sc[1]), __float_as_uint(sc[3]), false, false);
      const float m02 = fmax_raw(__uint_as_float(p02[0]), __uint_as_float(p02[1]));
      const float m13 = fmax_raw(__uint_as_float(p13[0]), __uint_as_float(p13[1]));
      auto pq = __builtin_amdgcn_permlane16_swap(__float_as_uint(m02), __float_as_uint(m13), false, false);
      float m = fmax_raw(__uint_as_float(pq[0]), __uint_as_float(pq[1]));
      m = fmax_raw(m, dppf<XOR1>(m)); m = fmax_raw(m, dppf<XOR2>(m)); m = fmax_raw(m, dppf<HMIR>(m)); m = fmax_raw(m, dppf<ROR8>(m));
      LAS u16* pw = (LAS u16*)(LK + LK_P);
#pragma unroll
      for (int r = 0; r < 4; ++r) {
        const float p = __builtin_amdgcn_exp2f((sc[r] - rdlane(m, 16 * r)) * LOG2E);
        pw[r * 64 + lane] = (u16)pk_bf16(p, p);
      }
      CBAR();
#pragma unroll
      for (int u = 0; u < 2; ++u) pf[u] = *(LAS v4u*)(LK + LK_P + (c >> 2) * 128 + u * 64 + g * 16);
      const v4u ones = {0x3f803f80u, 0x3f803f80u, 0x3f803f80u, 0x3f803f80u};
      f32x4 ls = mfma(pf[0], ones, f32x4{0.f, 0.f, 0.f, 0.f});
      ls = mfma(pf[1], ones, ls);
      if (c == 0) {
        *(LAS float*)(L + L_ML + g * 32 + wave * 4) = m;
        *(LAS float*)(L + L_ML + 128 + g * 32 + wave * 4) = ls[0];
      }
      lds_signal(&flags[F_SOFT]);
    }
    if (wave >= 2) V_LOADS();
    {
      f32x4 P0 = {0.f, 0.f, 0.f, 0.f}, P1 = P0;
      float wt7 = 0.f;
      int hdst = WS_H1 + (((kvh * 4 + g) * 8 + b) * 4 + s) * 512 + c * 16;
      PIN(hdst);
#define PRESUM(W0, W1) _Pragma("unroll") for (int w = (W0); w < (W1); ++w) { \
        const float wt = *(LAS float*)(L + L_WT + g * 32 + w * 4); \
        const f32x4 a0 = *(LAS f32x4*)(L + L_K + ((w + 2) & 7) * 4096 + LK_ACC + lane * 16), a1 = *(LAS f32x4*)(L + L_K + ((w + 2) & 7) * 4096 + LK_ACC + 1024 + lane * 16); \
        _Pragma("unroll") for (int k = 0; k < 4; ++k) { P0[k] = fmaf(wt, a0[k], P0[k]); P1[k] = fmaf(wt, a1[k], P1[k]); } }
      bool early = false, early6 = false;
      if (wave == 7) {
        __builtin_amdgcn_s_setprio(2);
#pragma unroll 1
        for (int i = 0; i < 4 && !early; ++i) early = (lds_get(&flags[F_WT]) != 0u) & (lds_get(&flags[F_ACC]) >= 5u);
        CBAR();
        if (early) {
          PRESUM(0, 5) PIN(P0); PIN(P1);
#pragma unroll 1
          for (int i = 0; i < TRY6 && !early6; ++i) early6 = lds_get(&flags[F_ACC6]) >= 2u;
          CBAR();
          if (early6) { PRESUM(5, 7) wt7 = *(LAS float*)(L + L_WT + g * 32 + 28); PIN(P0); PIN(P1); PIN(wt7); }
        }
      }
      f32x4 O[8];
      v4u Bt[8];
#define TRP(u, kp) _Pragma("unroll") for (int jp = 0; jp < 4; ++jp) { \
        const unsigned a_ = Vf[u][2 * (kp)][jp], b_ = Vf[u][2 * (kp) + 1][jp]; \
        Bt[2 * jp][kp] = __builtin_amdgcn_perm(b_, a_, 0x05040100u); Bt[2 * jp + 1][kp] = __builtin_amdgcn_perm(b_, a_, 0x07060302u); }
      TRP(0, 0) TRP(0, 1) TRP(0, 2) TRP(0, 3)
#pragma unroll
      for (int j = 0; j < 8; ++j) O[j] = mfma(pf[0], Bt[j], f32x4{0.f, 0.f, 0.f, 0.f});
      SCHED();
      if (__builtin_expect(wave == 7 && !early, 0)) {
#pragma unroll 1
        for (int i = 0; i < 3 && !early; ++i) early = (lds_get(&flags[F_WT]) != 0u) & (lds_get(&flags[F_ACC]) >= 5u);
        CBAR();
        if (early) { PRESUM(0, 5) PIN(P0); PIN(P1); }
      }
      if (early && !early6) {
#pragma unroll 1
        for (int i = 0; i < 8 && !early6; ++i) early6 = lds_get(&flags[F_ACC6]) >= 2u;
        CBAR();
        if (early6) { PRESUM(5, 7) wt7 = *(LAS float*)(L + L_WT + g * 32 + 28); PIN(P0); PIN(P1); PIN(wt7); }
      }
      VMCNT(6); lds_signal(&flags[F_NEAR], early6); SCHED();
      TRP(1, 0) TRP(1, 1) TRP(1, 2)
      SCHED();
      TRP(1, 3)
#pragma unroll
      for (int j = 0; j < 8; ++j) O[j] = mfma(pf[1], Bt[j], O[j]);
      if (__builtin_expect(wave == 7, 1)) {
        if (__builtin_expect(!early6, 0)) {
          lds_spin_ge(&flags[F_WT], 1u);
          if (!early) { lds_spin_ge(&flags[F_ACC], 5u); PRESUM(0, 5) }
          lds_spin_ge(&flags[F_ACC6], 2u);
          lds_signal(&flags[F_NEAR]);
          PRESUM(5, 7)
          wt7 = *(LAS float*)(L + L_WT + g * 32 + 28);
        }
        bst<AUX_SC1>(v4u{pk_bf16(fmaf(wt7, O[0][0], P0[0]), fmaf(wt7, O[1][0], P0[1])), tag, pk_bf16(fmaf(wt7, O[2][0], P0[2]), fmaf(wt7, O[3][0], P0[3])), tag}, rws, hdst);
        bst<AUX_SC1>(v4u{pk_bf16(fmaf(wt7, O[4][0], P1[0]), fmaf(wt7, O[5][0], P1[1])), tag, pk_bf16(fmaf(wt7, O[6][0], P1[2]), fmaf(wt7, O[7][0], P1[3])), tag}, rws, hdst + 256);
      } else {
        LAS unsigned char* ap = LK + LK_ACC + lane * 16;
        *(LAS f32x4*)(ap) = f32x4{O[0][0], O[1][0], O[2][0], O[3][0]};
        *(LAS f32x4*)(ap + 1024) = f32x4{O[4][0], O[5][0], O[6][0], O[7][0]};
        lds_signal(&flags[wave < 5 ? F_ACC : F_ACC6]);
      }
    }

    if (wave == 4) {
      const unsigned tl = lds_get(&flags[F_TL]) + wstart;
      lds_wait_ge<1>(&flags[F_ISSUE], 8u);
      W_LOADS(WE, 6, wait_until(tl + (j - WE) * wstep));
      VMCNT(0);
      lds_set(&flags[F_W2], 1u);
    } else if (wave < 4) {
      if (wave < 2) {
        const int hsrc = has_f ? WS_HG + fb * 8192 + wave * 1024 + lane * 16 : OOB;
        const unsigned want = has_f ? tag : 0u;
        v4u hp[4];
#pragma unroll 1
        for (unsigned it = 0; it < SPIN_G; ++it) {
          CBAR();
#pragma unroll
          for (int k = 0; k < 4; ++k) hp[k] = bld<AUX_SC1_NT>(rws, hsrc, k * 2048);
          if (__all(tag12_ok(hp[0], want) & tag12_ok(hp[1], want) & tag12_ok(hp[2], want) & tag12_ok(hp[3], want))) break;
        }
        const f32x4 hv = (unpack_f26(hp[0]) + unpack_f26(hp[1])) + (unpack_f26(hp[2]) + unpack_f26(hp[3]));
        *(LAS v2u*)(L + L_H + (wave * 4 + g) * 144 + c * 8) =
            v2u{pk_bf16(fmaxf(hv[0], 0.f), fmaxf(hv[1], 0.f)), pk_bf16(fmaxf(hv[2], 0.f), fmaxf(hv[3], 0.f))};
        lds_signal(&flags[F_W]);
      }
      lds_wait_ge<1>(&flags[F_W], 3u);
      LAS unsigned char* tb = L + L_W + (8 * g + (c >> 2)) * 128 + wave * 32 + (c & 3) * 8;
      v4u Wa[6];
#pragma unroll
      for (int j = 0; j < WE; ++j) { Wa[WGRP(j)] = tr_frag(tb + WGRP(j) * 4096, 512); PIN(Wa[WGRP(j)]); }
      f32x4 D = mfma(Wa[4], *(LAS v4u*)(L + L_H + (c & 7) * 144 + (fb & 1) * 64 + g * 16), f32x4{0.f, 0.f, 0.f, 0.f});
      D = mfma(Wa[5], *(LAS v4u*)(L + L_H + (c & 7) * 144 + (~fb & 1) * 64 + g * 16), D);
      PIN(D);
      int dst = c < 8 ? WS_H2 + (((cb8 * 2 + (wave >> 1)) * 8 + c) * 32 + h2) * 128 + ((wave & 1) * 4 + g) * 16 : OOB;
      int srcl = WS_ML + ((kvh * 8 + 2 * wave) * 4 + (lane & 7)) * 128 + (h2 & 3) * 16;
      int src = WS_H1 + (h2 * 8 + 2 * wave) * 2048 + lane * 16;
      int owr = L_O + (2 * wave + (lane >> 5)) * 272 + (c ^ (wrot * 4)) * 16 + (lane & 16) / 2, ord = L_O + (c & 7) * 272 + g * 16;
      PIN(dst); PIN(srcl); PIN(src); PIN(owr); PIN(ord);
      const unsigned t_ml = lds_get(&flags[F_TL]) + wdelay;
      wait_until(t_ml - hdelay - 12);
      lds_spin_ge(&flags[F_NEAR], 1u);
      spin_until(clk32() + hdelay);
      spin_until(t_ml);
      v4u ml = bld<AUX_SC1>(rws, srcl, 0), u[4];
      if (__builtin_expect(__builtin_amdgcn_ballot_w64((ml.y & ml.w & tag) != (ml.y | ml.w | tag)) != 0ull, 0)) {
#pragma unroll 1
        for (unsigned it = 0; it < SPIN_G; ++it) {
          CBAR();
          ml = bld<AUX_SC1>(rws, srcl, 0);
          if (__all(tag_ok(ml, tag))) break;
        }
      }
#pragma unroll
      for (int k = 0; k < 4; ++k) u[k] = bld<AUX_SC1>(rws, src + k * 1024, 0);
      SCHED();
      const float e = __uint_as_float(ml.x);
      float M = fmax_raw(e, dppf<XOR1>(e)); M = fmax_raw(M, dppf<XOR2>(M));
      float w = __builtin_amdgcn_exp2f((e - M) * LOG2E) * __uint_as_float(ml.z);
      float Ls = w + dppf<XOR1>(w); Ls += dppf<XOR2>(Ls);
      w *= __builtin_amdgcn_rcpf(Ls);
      const float wx = dppf<XOR1>(w);
      w = (lane & 32) ? wx : w;
      const float wa = dppf<0x00>(w), wb = dppf<0xAA>(w);
      const float wc = dppf<HMIR>(wa), wd = dppf<HMIR>(wb);
      float wk[4] = {(lane & 4) ? wc : wa, (lane & 4) ? wd : wb, (lane & 4) ? wa : wc, (lane & 4) ? wb : wd};
      PIN(wk[0]); PIN(wk[1]); PIN(wk[2]); PIN(wk[3]);
      SCHED();
      lds_spin_ge(&flags[F_W2], 1u);
#pragma unroll
      for (int j = WE; j < 6; ++j) Wa[WGRP(j)] = tr_frag(tb + WGRP(j) * 4096, 512);
#pragma unroll
      for (int j = WE; j < 6; ++j) PIN(Wa[WGRP(j)]);
      SCHED();
      if (__builtin_expect(!all4_ok(u, tag), 0)) {
#pragma unroll 1
        for (unsigned it = 0; it < SPIN_G; ++it) {
          CBAR();
#pragma unroll
          for (int k = 0; k < 4; ++k) u[k] = bld<AUX_SC1>(rws, src + k * 1024, 0);
          if (all4_ok(u, tag)) break;
        }
      }
      const float o0 = fold32(fmaf(wk[0], bflo(u[0].x), wk[1] * bflo(u[1].x)), fmaf(wk[2], bflo(u[2].x), wk[3] * bflo(u[3].x)));
      const float o1 = fold32(fmaf(wk[0], bfhi(u[0].x), wk[1] * bfhi(u[1].x)), fmaf(wk[2], bfhi(u[2].x), wk[3] * bfhi(u[3].x)));
      const float o2 = fold32(fmaf(wk[0], bflo(u[0].z), wk[1] * bflo(u[1].z)), fmaf(wk[2], bflo(u[2].z), wk[3] * bflo(u[3].z)));
      const float o3 = fold32(fmaf(wk[0], bfhi(u[0].z), wk[1] * bfhi(u[1].z)), fmaf(wk[2], bfhi(u[2].z), wk[3] * bfhi(u[3].z)));
      *(LAS v2u*)(L + owr) = v2u{pk_bf16(o0, o1), pk_bf16(o2, o3)};
      lds_signal(&flags[F_O]);
      lds_spin_ge(&flags[F_O], 4u);
      v4u of[4];
#pragma unroll
      for (int ks = 0; ks < 4; ++ks) of[ks] = *(LAS v4u*)(L + ord + ks * 64);
      SCHED();
#pragma unroll
      for (int ks = 0; ks < 4; ++ks) D = mfma(Wa[ks], of[ks], D);
      bst<AUX_SC1>(v4u{pk_bf16(D[0], D[1]), tag, pk_bf16(D[2], D[3]), tag}, rws, dst);
    }
  }
}

extern "C" void kernel_launch(void* const* d_in, const int* in_sizes, int n_in, void* d_out, int out_size, void* d_ws,
                              size_t ws_size, hipStream_t stream) {
  fused_kernel<<<256, NWAVE * 64, 0, stream>>>((const u16*)d_in[0], (const u16*)d_in[1], (const u16*)d_in[2], (const u16*)d_in[3],
                                               (const u16*)d_in[4], (const u16*)d_in[6], (const u16*)d_in[7], (u16*)d_out,
                                               (unsigned char*)d_ws);
}
```
